```python
import torch

def _softmax(x, axis=-1):
    return torch.softmax(x, dim=axis)
def _gelu(x):
    return torch.nn.functional.gelu(x, approximate="tanh")
def _silu(x):
    return torch.nn.functional.silu(x)
def _sigmoid(x):
    return torch.sigmoid(x)
def _layernorm(x, axis=-1, eps=1e-5):
    m = x.mean(dim=axis, keepdim=True)
    v = x.var(dim=axis, keepdim=True, unbiased=False)
    return (x - m) / torch.sqrt(v + eps)
def _scatter_add(tgt, idx, src, axis=0):
    return tgt.clone().index_add_(axis, idx, src)
def _segment_sum(src, seg, n):
    out = torch.zeros((n,) + src.shape[1:], dtype=src.dtype)
    return out.index_add_(0, seg, src)
def _pad(x, pw):
    flat = tuple(v for pair in reversed(pw) for v in pair)
    return torch.nn.functional.pad(x, flat)

kernel_name = 'standalone__standard_2d_1d_3d_conv__vision'

def setup_inputs():

    _w = (lambda a: (lambda t: t.to(torch.bfloat16) if t.is_floating_point() else t)(torch.as_tensor(a)))
    H = W = 256
    C = 384
    K = 1
    return {
        "x": _w((lambda *s: torch.randn(*s, dtype=torch.bfloat16) * 0.05)(1, H, W, C)),
        "w": _w((lambda *s: torch.randn(*s, dtype=torch.bfloat16) * 0.05)(K, K, C, C)),
        "b": _w((lambda *s: torch.randn(*s, dtype=torch.bfloat16) * 0.05)(C)),
    }

def reference(x, w, b):
    B, H, W, Cin = x.shape
    K = w.shape[0]
    pl = (K - 1) // 2
    pr = K // 2
    xp = _pad(x, ((0, 0), (pl, pr), (pl, pr), (0, 0)))
    out = sum(
        torch.einsum('bhwc,cd->bhwd', xp[:, i:i+H, j:j+W, :], w[i, j])
        for i in range(K) for j in range(K)
    )
    return out + b[None, None, None, :]
```

```cpp
#include <hip/hip_runtime.h>
#include <hip/hip_bf16.h>
#include <stdint.h>

typedef __attribute__((ext_vector_type(8))) short bf16x8;
typedef __attribute__((ext_vector_type(4))) float f32x4;
typedef __attribute__((ext_vector_type(2))) float f32x2;
typedef __attribute__((ext_vector_type(2))) __bf16 bf16x2;
typedef __attribute__((ext_vector_type(4))) uint32_t u32x4;
typedef uint32_t u32x3 __attribute__((ext_vector_type(3), aligned(4)));

#define CC 384
#define ROWB 768
#define TROWS 32
#define TILEB (TROWS * ROWB)
#define NXBUF 4
#define WG_ROWS 256
#define NTILES (WG_ROWS / TROWS)
#define LDS_BYTES ((NXBUF + 2) * TILEB)

__device__ __forceinline__ uint32_t cvt_pk_bf16(float a, float b) {
    f32x2 v = {a, b};
    bf16x2 h = __builtin_convertvector(v, bf16x2);
    return __builtin_bit_cast(uint32_t, h);
}

__device__ __forceinline__ void glds16(const void* sbase, uint32_t voff, uint32_t lds_dst) {
    uint32_t keep;
    asm volatile(
        "s_mov_b32 %0, m0\n\t"
        "s_mov_b32 m0, %3\n\t"
        "s_nop 0\n\t"
        "global_load_lds_dwordx4 %1, %2\n\t"
        "s_mov_b32 m0, %0"
        : "=&s"(keep)
        : "v"(voff), "s"(sbase), "s"(lds_dst)
        : "memory");
}

__global__ __launch_bounds__(256, 1) void conv1x1_kernel(const char* __restrict__ x, const char* __restrict__ w,
                                                         const char* __restrict__ bias, char* __restrict__ out) {
    __shared__ __attribute__((aligned(1024))) char smem[LDS_BYTES];
    const int tid = threadIdx.x;
    const int lane = tid & 63;
    const int wv = __builtin_amdgcn_readfirstlane(tid >> 6);
    const int q = lane >> 4, r = lane & 15;
    const uint32_t lds0 = (uint32_t)(size_t)(__attribute__((address_space(3))) char*)smem;

    uint32_t so[6];
#pragma unroll
    for (int m = 0; m < 6; ++m) {
        int p = 64 * m + lane;
        int rowl = p / 48, c = p % 48;
        int rr = 8 * (wv & 1) + rowl;
        so[m] = rowl * ROWB + ((c ^ rr) * 16);
    }
    const size_t wg_off = (size_t)blockIdx.x * WG_ROWS * ROWB + (size_t)wv * 8 * ROWB;
    const char* xw = x + wg_off;
    char* ow = out + wg_off;

    auto issue_dma = [&](int t) {
        const char* src = xw + (size_t)t * TILEB;
        uint32_t dst = lds0 + (t & (NXBUF - 1)) * TILEB + wv * 6144;
#pragma unroll
        for (int m = 0; m < 6; ++m) glds16(src, so[m], dst + m * 1024);
    };

    bf16x8 Wf[12][6];
    const char* wl = w + (size_t)(8 * q) * ROWB + 192 * wv + 12 * r;
    auto load_w = [&](int s) {
        u32x3 v[8];
#pragma unroll
        for (int j = 0; j < 8; ++j) v[j] = *(const u32x3*)(wl + (size_t)(32 * s + j) * ROWB);
#pragma unroll
        for (int tt = 0; tt < 6; ++tt) {
            u32x4 f;
#pragma unroll
            for (int m = 0; m < 4; ++m) {
                uint32_t a = v[2 * m][tt >> 1], b = v[2 * m + 1][tt >> 1];
                f[m] = (tt & 1) ? __builtin_amdgcn_perm(b, a, 0x07060302u) : __builtin_amdgcn_perm(b, a, 0x05040100u);
            }
            Wf[s][tt] = __builtin_bit_cast(bf16x8, f);
        }
    };

    issue_dma(0);
#pragma unroll
    for (int s = 0; s < 4; ++s) load_w(s);
    issue_dma(1);
#pragma unroll
    for (int s = 4; s < 8; ++s) load_w(s);
    issue_dma(2);
#pragma unroll
    for (int s = 8; s < 12; ++s) load_w(s);
    issue_dma(3);

    float bv[24];
    {
        const u32x4* bp = (const u32x4*)(bias + 192 * wv + 48 * q);
#pragma unroll
        for (int u = 0; u < 3; ++u) {
            u32x4 t4 = bp[u];
#pragma unroll
            for (int e = 0; e < 4; ++e) {
                bv[8 * u + 2 * e] = __uint_as_float(t4[e] << 16);
                bv[8 * u + 2 * e + 1] = __uint_as_float(t4[e] & 0xffff0000u);
            }
        }
    }

    uint32_t rdb[4], wrb[3];
#pragma unroll
    for (int j = 0; j < 4; ++j) rdb[j] = r * ROWB + (((4 * j + q) ^ r) * 16);
#pragma unroll
    for (int u = 0; u < 3; ++u) wrb[u] = r * ROWB + (((12 * wv + 3 * q + u) ^ r) * 16);

    asm volatile("s_waitcnt vmcnt(63)" ::: "memory");
    __builtin_amdgcn_s_barrier();

    auto store_out = [&](int t) {
        const char* ob = smem + (NXBUF + (t & 1)) * TILEB + wv * 6144 + lane * 16;
        char* dst = ow + (size_t)t * TILEB;
        u32x4 v[6];
#pragma unroll
        for (int m = 0; m < 6; ++m) v[m] = *(const u32x4*)(ob + m * 1024);
#pragma unroll
        for (int m = 0; m < 6; ++m) *(u32x4*)(dst + so[m]) = v[m];
    };

#pragma unroll 1
    for (int t = 0; t < NTILES; ++t) {
        if (t >= 1) {
            if (t + 3 < NTILES) issue_dma(t + 3);
            store_out(t - 1);
        }
        const char* xb = smem + (t & (NXBUF - 1)) * TILEB;
        f32x4 acc[2][6];
#pragma unroll
        for (int mb = 0; mb < 2; ++mb)
#pragma unroll
            for (int tt = 0; tt < 6; ++tt) acc[mb][tt] = (f32x4){0.f, 0.f, 0.f, 0.f};
#pragma unroll
        for (int s = 0; s < 12; ++s) {
            bf16x8 b0 = *(const bf16x8*)(xb + rdb[s & 3] + (s >> 2) * 256);
            bf16x8 b1 = *(const bf16x8*)(xb + rdb[s & 3] + (s >> 2) * 256 + 16 * ROWB);
#pragma unroll
            for (int tt = 0; tt < 6; ++tt) {
                acc[0][tt] = __builtin_amdgcn_mfma_f32_16x16x32_bf16(Wf[s][tt], b0, acc[0][tt], 0, 0, 0);
                acc[1][tt] = __builtin_amdgcn_mfma_f32_16x16x32_bf16(Wf[s][tt], b1, acc[1][tt], 0, 0, 0);
            }
        }
        char* ob = smem + (NXBUF + (t & 1)) * TILEB;
#pragma unroll
        for (int mb = 0; mb < 2; ++mb) {
            uint32_t d[12];
#pragma unroll
            for (int g = 0; g < 4; ++g)
#pragma unroll
                for (int h = 0; h < 3; ++h) {
                    uint32_t p = cvt_pk_bf16(acc[mb][2 * h][g], acc[mb][2 * h + 1][g]);
                    float lo = __uint_as_float(p << 16) + bv[6 * g + 2 * h];
                    float hi = __uint_as_float(p & 0xffff0000u) + bv[6 * g + 2 * h + 1];
                    d[3 * g + h] = cvt_pk_bf16(lo, hi);
                }
#pragma unroll
            for (int u = 0; u < 3; ++u) {
                u32x4 v = {d[4 * u], d[4 * u + 1], d[4 * u + 2], d[4 * u + 3]};
                *(u32x4*)(ob + wrb[u] + mb * 16 * ROWB) = v;
            }
        }
        if (t == 0)
            asm volatile("s_waitcnt vmcnt(12) lgkmcnt(0)" ::: "memory");
        else if (t == 1 || t == 6)
            asm volatile("s_waitcnt vmcnt(18) lgkmcnt(0)" ::: "memory");
        else if (t == 2 || t == 5)
            asm volatile("s_waitcnt vmcnt(24) lgkmcnt(0)" ::: "memory");
        else if (t == 3 || t == 4)
            asm volatile("s_waitcnt vmcnt(30) lgkmcnt(0)" ::: "memory");
        else
            asm volatile("s_waitcnt lgkmcnt(0)" ::: "memory");
        __builtin_amdgcn_s_barrier();
    }
    store_out(NTILES - 1);
}

__global__ void conv1x1_fallback(const __hip_bfloat16* __restrict__ x, const __hip_bfloat16* __restrict__ w,
                                 const __hip_bfloat16* __restrict__ b, __hip_bfloat16* __restrict__ out, int M) {
    int n = threadIdx.x;
    for (int row = blockIdx.x; row < M; row += gridDim.x) {
        float acc = 0.f;
        for (int k = 0; k < CC; ++k) acc += __bfloat162float(x[(size_t)row * CC + k]) * __bfloat162float(w[k * CC + n]);
        float t = __bfloat162float(__float2bfloat16(acc)) + __bfloat162float(b[n]);
        out[(size_t)row * CC + n] = __float2bfloat16(t);
    }
}

extern "C" void kernel_launch(void* const* d_in, const int* in_sizes, int n_in, void* d_out, int out_size, void* d_ws,
                              size_t ws_size, hipStream_t stream) {
    const int M = in_sizes[0] / CC;
    if (M % WG_ROWS == 0) {
        conv1x1_kernel<<<M / WG_ROWS, 256, 0, stream>>>((const char*)d_in[0], (const char*)d_in[1],
                                                        (const char*)d_in[2], (char*)d_out);
    } else {
        conv1x1_fallback<<<2048, CC, 0, stream>>>((const __hip_bfloat16*)d_in[0], (const __hip_bfloat16*)d_in[1],
                                                  (const __hip_bfloat16*)d_in[2], (__hip_bfloat16*)d_out, M);
    }
}
```

```cpp
#include <hip/hip_runtime.h>
#include <hip/hip_bf16.h>
#include <stdint.h>

typedef __attribute__((ext_vector_type(8))) short bf16x8;
typedef __attribute__((ext_vector_type(4))) float f32x4;
typedef __attribute__((ext_vector_type(2))) float f32x2;
typedef __attribute__((ext_vector_type(2))) __bf16 bf16x2;
typedef __attribute__((ext_vector_type(4))) uint32_t u32x4;
typedef uint32_t u32x3 __attribute__((ext_vector_type(3), aligned(4)));

#define CC 384
#define ROWB 768
#define TROWS 32
#define TILEB (TROWS * ROWB)
#define NXBUF 4
#define WG_ROWS 256
#define NTILES (WG_ROWS / TROWS)
#define LDS_BYTES ((NXBUF + 2) * TILEB)

__device__ __forceinline__ uint32_t cvt_pk_bf16(float a, float b) {
    f32x2 v = {a, b};
    bf16x2 h = __builtin_convertvector(v, bf16x2);
    return __builtin_bit_cast(uint32_t, h);
}

__device__ __forceinline__ void glds16(const void* sbase, uint32_t voff, uint32_t lds_dst) {
    uint32_t keep;
    asm volatile(
        "s_mov_b32 %0, m0\n\t"
        "s_mov_b32 m0, %3\n\t"
        "s_nop 0\n\t"
        "global_load_lds_dwordx4 %1, %2 nt\n\t"
        "s_mov_b32 m0, %0"
        : "=&s"(keep)
        : "v"(voff), "s"(sbase), "s"(lds_dst)
        : "memory");
}

__device__ __forceinline__ void gstore16(void* sbase, uint32_t voff, u32x4 v) {
    asm volatile("global_store_dwordx4 %0, %1, %2 sc0 sc1 nt\n\ts_nop 1" ::"v"(voff), "v"(v), "s"(sbase) : "memory");
}

__global__ __launch_bounds__(512, 2) void conv1x1_kernel(const char* __restrict__ x, const char* __restrict__ w,
                                                         const char* __restrict__ bias, char* __restrict__ out) {
    __shared__ __attribute__((aligned(1024))) char smem[LDS_BYTES];
    const int tid = threadIdx.x;
    const int lane = tid & 63;
    const int wv = __builtin_amdgcn_readfirstlane(tid >> 6);
    const int pp = wv >> 1, hh = wv & 1;
    const int q = lane >> 4, r = lane & 15;
    const uint32_t lds0 = (uint32_t)(size_t)(__attribute__((address_space(3))) char*)smem;

    float bv[4][3];
    {
        const uint16_t* bp = (const uint16_t*)bias + 96 * pp + 24 * q;
#pragma unroll
        for (int g = 0; g < 4; ++g) {
            bv[g][0] = __uint_as_float((uint32_t)bp[6 * g + 4 * hh] << 16);
            bv[g][1] = __uint_as_float((uint32_t)bp[6 * g + 4 * hh + 1] << 16);
            bv[g][2] = __uint_as_float((uint32_t)bp[6 * g + 2 + hh] << 16);
        }
    }

    uint32_t so[3];
#pragma unroll
    for (int m = 0; m < 3; ++m) {
        int p = 64 * m + lane;
        int rowl = p / 48, c = p % 48;
        int rr = 4 * (wv & 3) + rowl;
        so[m] = rowl * ROWB + ((c ^ rr) * 16);
    }
    const size_t wg_off = (size_t)blockIdx.x * WG_ROWS * ROWB + (size_t)wv * 4 * ROWB;
    const char* xw = x + wg_off;
    char* ow = out + wg_off;

    auto issue_dma = [&](int t) {
        const char* src = xw + (size_t)t * TILEB;
        uint32_t dst = lds0 + (t & (NXBUF - 1)) * TILEB + wv * 3072;
#pragma unroll
        for (int m = 0; m < 3; ++m) glds16(src, so[m], dst + m * 1024);
    };

    issue_dma(0);
    u32x3 wr[6][8];
    {
        const char* wl = w + (size_t)(192 * hh + 8 * q) * ROWB + 192 * pp + 12 * r;
#pragma unroll
        for (int rho = 0; rho < 6; ++rho)
#pragma unroll
            for (int j = 0; j < 8; ++j) wr[rho][j] = *(const u32x3*)(wl + (size_t)(32 * rho + j) * ROWB);
    }
    issue_dma(1);
    issue_dma(2);
    issue_dma(3);

    uint32_t rdb[4];
#pragma unroll
    for (int j = 0; j < 4; ++j) rdb[j] = r * ROWB + (((4 * j + q) ^ r) * 16);

    bf16x8 Wf[12][3];
    f32x4 acc[2][3];
#pragma unroll
    for (int mb = 0; mb < 2; ++mb)
#pragma unroll
        for (int u = 0; u < 3; ++u) acc[mb][u] = (f32x4){0.f, 0.f, 0.f, 0.f};

#pragma unroll
    for (int rho = 0; rho < 6; ++rho) {
        u32x4 own[3], oth[3];
#pragma unroll
        for (int tt = 0; tt < 6; ++tt) {
            u32x4 f;
#pragma unroll
            for (int m = 0; m < 4; ++m) {
                uint32_t a = wr[rho][2 * m][tt >> 1], b = wr[rho][2 * m + 1][tt >> 1];
                f[m] = (tt & 1) ? __builtin_amdgcn_perm(b, a, 0x07060302u) : __builtin_amdgcn_perm(b, a, 0x05040100u);
            }
            if (tt < 3) { own[tt] = f; oth[tt] = f; }
            else {
                own[tt - 3] = hh ? f : own[tt - 3];
                oth[tt - 3] = hh ? oth[tt - 3] : f;
            }
        }
        char* eb = smem + (NXBUF + (rho & 1)) * TILEB;
#pragma unroll
        for (int u = 0; u < 3; ++u) *(u32x4*)(eb + wv * 3072 + u * 1024 + lane * 16) = oth[u];
        asm volatile("s_waitcnt lgkmcnt(0)" ::: "memory");
        __builtin_amdgcn_s_barrier();
#pragma unroll
        for (int u = 0; u < 3; ++u) {
            u32x4 rc = *(const u32x4*)(eb + (wv ^ 1) * 3072 + u * 1024 + lane * 16);
            Wf[rho][u] = __builtin_bit_cast(bf16x8, hh ? rc : own[u]);
            Wf[6 + rho][u] = __builtin_bit_cast(bf16x8, hh ? own[u] : rc);
        }
#pragma unroll
        for (int hs = 0; hs < 2; ++hs) {
            const int s = 6 * hs + rho;
            bf16x8 b0 = *(const bf16x8*)(smem + rdb[s & 3] + (s >> 2) * 256);
            bf16x8 b1 = *(const bf16x8*)(smem + rdb[s & 3] + (s >> 2) * 256 + 16 * ROWB);
#pragma unroll
            for (int u = 0; u < 3; ++u) {
                acc[0][u] = __builtin_amdgcn_mfma_f32_16x16x32_bf16(Wf[s][u], b0, acc[0][u], 0, 0, 0);
                acc[1][u] = __builtin_amdgcn_mfma_f32_16x16x32_bf16(Wf[s][u], b1, acc[1][u], 0, 0, 0);
            }
        }
    }
    asm volatile("s_waitcnt lgkmcnt(0)" ::: "memory");
    __builtin_amdgcn_s_barrier();

    uint32_t wa[4], wb[4];
#pragma unroll
    for (int g = 0; g < 4; ++g) {
        int ba = 192 * pp + 48 * q + 12 * g + 8 * hh;
        int bb = 192 * pp + 48 * q + 12 * g + 4 + 2 * hh;
        wa[g] = r * ROWB + ((((ba >> 4) ^ r) << 4) | (ba & 15));
        wb[g] = r * ROWB + ((((bb >> 4) ^ r) << 4) | (bb & 15));
    }

#pragma unroll 1
    for (int t = 0; t < NTILES; ++t) {
        char* ob = smem + (NXBUF + (t & 1)) * TILEB;
#pragma unroll
        for (int mb = 0; mb < 2; ++mb) {
#pragma unroll
            for (int g = 0; g < 4; ++g) {
                float v0 = acc[mb][0][g], v1 = acc[mb][1][g], v2 = acc[mb][2][g];
                float a0 = hh ? v1 : v0, a1 = hh ? v2 : v1, b0 = hh ? v0 : v2;
                uint32_t pa = cvt_pk_bf16(a0, a1);
                uint32_t pb = cvt_pk_bf16(b0, b0);
                float lo = __uint_as_float(pa << 16) + bv[g][0];
                float hi = __uint_as_float(pa & 0xffff0000u) + bv[g][1];
                float bb = __uint_as_float(pb & 0xffff0000u) + bv[g][2];
                *(uint32_t*)(ob + wa[g] + mb * 16 * ROWB) = cvt_pk_bf16(lo, hi);
                *(uint16_t*)(ob + wb[g] + mb * 16 * ROWB) = (uint16_t)cvt_pk_bf16(bb, bb);
            }
        }
        if (t == 0)
            asm volatile("s_waitcnt vmcnt(6) lgkmcnt(0)" ::: "memory");
        else if (t == 1 || t == 6)
            asm volatile("s_waitcnt vmcnt(9) lgkmcnt(0)" ::: "memory");
        else if (t == 2 || t == 5)
            asm volatile("s_waitcnt vmcnt(12) lgkmcnt(0)" ::: "memory");
        else if (t == 3 || t == 4)
            asm volatile("s_waitcnt vmcnt(15) lgkmcnt(0)" ::: "memory");
        else
            asm volatile("s_waitcnt lgkmcnt(0)" ::: "memory");
        __builtin_amdgcn_s_barrier();

        if (t + NXBUF < NTILES) issue_dma(t + NXBUF);
        {
            const char* sb = ob + wv * 3072 + lane * 16;
            char* dst = ow + (size_t)t * TILEB;
            u32x4 v[3];
#pragma unroll
            for (int m = 0; m < 3; ++m) v[m] = *(const u32x4*)(sb + m * 1024);
#pragma unroll
            for (int m = 0; m < 3; ++m) gstore16(dst, so[m], v[m]);
        }
        if (t + 1 < NTILES) {
            const char* xb = smem + ((t + 1) & (NXBUF - 1)) * TILEB;
#pragma unroll
            for (int mb = 0; mb < 2; ++mb)
#pragma unroll
                for (int u = 0; u < 3; ++u) acc[mb][u] = (f32x4){0.f, 0.f, 0.f, 0.f};
#pragma unroll
            for (int s = 0; s < 12; ++s) {
                bf16x8 b0 = *(const bf16x8*)(xb + rdb[s & 3] + (s >> 2) * 256);
                bf16x8 b1 = *(const bf16x8*)(xb + rdb[s & 3] + (s >> 2) * 256 + 16 * ROWB);
#pragma unroll
                for (int u = 0; u < 3; ++u) {
                    acc[0][u] = __builtin_amdgcn_mfma_f32_16x16x32_bf16(Wf[s][u], b0, acc[0][u], 0, 0, 0);
                    acc[1][u] = __builtin_amdgcn_mfma_f32_16x16x32_bf16(Wf[s][u], b1, acc[1][u], 0, 0, 0);
                }
            }
        }
    }
}

__global__ void conv1x1_fallback(const __hip_bfloat16* __restrict__ x, const __hip_bfloat16* __restrict__ w,
                                 const __hip_bfloat16* __restrict__ b, __hip_bfloat16* __restrict__ out, int M) {
    int n = threadIdx.x;
    for (int row = blockIdx.x; row < M; row += gridDim.x) {
        float acc = 0.f;
        for (int k = 0; k < CC; ++k) acc += __bfloat162float(x[(size_t)row * CC + k]) * __bfloat162float(w[k * CC + n]);
        float t = __bfloat162float(__float2bfloat16(acc)) + __bfloat162float(b[n]);
        out[(size_t)row * CC + n] = __float2bfloat16(t);
    }
}

extern "C" void kernel_launch(void* const* d_in, const int* in_sizes, int n_in, void* d_out, int out_size, void* d_ws,
                              size_t ws_size, hipStream_t stream) {
    const int M = in_sizes[0] / CC;
    if (M % WG_ROWS == 0) {
        conv1x1_kernel<<<M / WG_ROWS, 512, 0, stream>>>((const char*)d_in[0], (const char*)d_in[1],
                                                        (const char*)d_in[2], (char*)d_out);
    } else {
        conv1x1_fallback<<<2048, CC, 0, stream>>>((const __hip_bfloat16*)d_in[0], (const __hip_bfloat16*)d_in[1],
                                                  (const __hip_bfloat16*)d_in[2], (__hip_bfloat16*)d_out, M);
    }
}
```

```cpp
#include <hip/hip_runtime.h>
#include <hip/hip_bf16.h>
#include <stdint.h>

typedef __attribute__((ext_vector_type(8))) short bf16x8;
typedef __attribute__((ext_vector_type(4))) float f32x4;
typedef __attribute__((ext_vector_type(2))) float f32x2;
typedef __attribute__((ext_vector_type(2))) __bf16 bf16x2;
typedef __attribute__((ext_vector_type(4))) uint32_t u32x4;
typedef __attribute__((ext_vector_type(2))) uint32_t u32x2;

#define CC 384
#define ROWB 768
#define TROWS 32
#define TILEB (TROWS * ROWB)
#define NXBUF 4
#define WG_ROWS 256
#define NTILES (WG_ROWS / TROWS)
#define LDS_BYTES ((NXBUF + 2) * TILEB)

__device__ __forceinline__ uint32_t cvt_pk_bf16(float a, float b) {
    f32x2 v = {a, b};
    bf16x2 h = __builtin_convertvector(v, bf16x2);
    return __builtin_bit_cast(uint32_t, h);
}

__device__ __forceinline__ void glds16_nt(const void* sbase, uint32_t voff, uint32_t lds_dst) {
    uint32_t keep;
    asm volatile(
        "s_mov_b32 %0, m0\n\t"
        "s_mov_b32 m0, %3\n\t"
        "s_nop 0\n\t"
        "global_load_lds_dwordx4 %1, %2 nt\n\t"
        "s_mov_b32 m0, %0"
        : "=&s"(keep)
        : "v"(voff), "s"(sbase), "s"(lds_dst)
        : "memory");
}
__device__ __forceinline__ void glds16(const void* sbase, uint32_t voff, uint32_t lds_dst) {
    uint32_t keep;
    asm volatile(
        "s_mov_b32 %0, m0\n\t"
        "s_mov_b32 m0, %3\n\t"
        "s_nop 0\n\t"
        "global_load_lds_dwordx4 %1, %2\n\t"
        "s_mov_b32 m0, %0"
        : "=&s"(keep)
        : "v"(voff), "s"(sbase), "s"(lds_dst)
        : "memory");
}

__device__ __forceinline__ void gstore16(void* sbase, uint32_t voff, u32x4 v) {
    asm volatile("global_store_dwordx4 %0, %1, %2 nt\n\ts_nop 1" ::"v"(voff), "v"(v), "s"(sbase) : "memory");
}

__global__ __launch_bounds__(512, 2) void conv1x1_kernel(const char* __restrict__ x, const char* __restrict__ w,
                                                         const char* __restrict__ bias, char* __restrict__ out) {
    __shared__ __attribute__((aligned(1024))) char smem[LDS_BYTES];
    const int tid = threadIdx.x;
    const int lane = tid & 63;
    const int wv = __builtin_amdgcn_readfirstlane(tid >> 6);
    const int q = lane >> 4, r = lane & 15;
    const uint32_t lds0 = (uint32_t)(size_t)(__attribute__((address_space(3))) char*)smem;

    uint32_t pref = 0;
    {
        int line = (wv & 1) * 64 + lane;
        line = line < 72 ? line : 71;
        const char* pa = w + ((blockIdx.x >> 3) & 31) * 9216 + line * 128;
        if (wv < 2) asm volatile("global_load_dword %0, %1, off" : "=v"(pref) : "v"(pa) : "memory");
    }
    u32x2 braw[3];
    {
        const char* bp = bias + 96 * wv + 24 * q;
        asm volatile(
            "global_load_dwordx2 %0, %3, off\n\tglobal_load_dwordx2 %1, %3, off offset:8\n\t"
            "global_load_dwordx2 %2, %3, off offset:16"
            : "=&v"(braw[0]), "=&v"(braw[1]), "=&v"(braw[2])
            : "v"(bp)
            : "memory");
    }
    f32x4 bvec[3];

    uint32_t so[3];
#pragma unroll
    for (int m = 0; m < 3; ++m) {
        int p = 64 * m + lane;
        int rowl = p / 48, c = p % 48;
        int rr = 4 * (wv & 3) + rowl;
        so[m] = rowl * ROWB + ((c ^ rr) * 16);
    }
    const size_t wg_off = (size_t)blockIdx.x * WG_ROWS * ROWB + (size_t)wv * 4 * ROWB;
    const char* xw = x + wg_off;
    char* ow = out + wg_off;
    const char* ww = w + wv * 4 * ROWB;

    auto issue_dma = [&](int t) {
        const char* src = xw + (size_t)t * TILEB;
        uint32_t dst = lds0 + (t & (NXBUF - 1)) * TILEB + wv * 3072;
#pragma unroll
        for (int m = 0; m < 3; ++m) glds16_nt(src, so[m], dst + m * 1024);
    };
    auto issue_w = [&](int c) {
        const char* src = ww + (size_t)c * TILEB;
        uint32_t dst = lds0 + (2 + (c & 3)) * TILEB + wv * 3072;
#pragma unroll
        for (int m = 0; m < 3; ++m) glds16(src, so[m], dst + m * 1024);
    };

    issue_dma(0);
    issue_dma(1);
    issue_w(0);
    issue_w(1);
    issue_w(2);

    uint32_t rdb[4];
#pragma unroll
    for (int j = 0; j < 4; ++j) rdb[j] = lds0 + r * ROWB + (((4 * j + q) ^ r) * 16);
    uint32_t tra[3][2];
    {
        const int a = r >> 2, p = r & 3;
#pragma unroll
        for (int u = 0; u < 3; ++u)
#pragma unroll
            for (int hf = 0; hf < 2; ++hf) {
                int k = 8 * q + 4 * hf + a;
                int b = 96 * wv + 24 * p + 8 * u;
                tra[u][hf] = lds0 + 2 * TILEB + k * ROWB + ((((b >> 4) ^ (k & 15)) << 4) | (b & 15));
            }
    }

    bf16x8 Wf[12][3];
    f32x4 acc[2][3];
#pragma unroll
    for (int c = 0; c < 12; ++c) {
        asm volatile("s_waitcnt vmcnt(6) lgkmcnt(0)" ::: "memory");
        __builtin_amdgcn_s_barrier();
        if (c == 0) {
            asm volatile("" : "+v"(braw[0]), "+v"(braw[1]), "+v"(braw[2]), "+v"(pref));
#pragma unroll
            for (int u = 0; u < 3; ++u) {
                bvec[u][0] = __uint_as_float(braw[u][0] << 16);
                bvec[u][1] = __uint_as_float(braw[u][0] & 0xffff0000u);
                bvec[u][2] = __uint_as_float(braw[u][1] << 16);
                bvec[u][3] = __uint_as_float(braw[u][1] & 0xffff0000u);
            }
        }
        if (c + 3 < 12) issue_w(c + 3);
        else if (c == 9 || c == 10) issue_dma(c - 7);
        u32x2 lo[3], hi[3];
        bf16x8 b0, b1;
        asm volatile(
            "ds_read_b64_tr_b16 %0, %8 offset:%14\n\t"
            "ds_read_b64_tr_b16 %1, %9 offset:%14\n\t"
            "ds_read_b64_tr_b16 %2, %10 offset:%14\n\t"
            "ds_read_b64_tr_b16 %3, %11 offset:%14\n\t"
            "ds_read_b64_tr_b16 %4, %12 offset:%14\n\t"
            "ds_read_b64_tr_b16 %5, %13 offset:%14\n\t"
            "ds_read_b128 %6, %15 offset:%16\n\t"
            "ds_read_b128 %7, %15 offset:%17\n\t"
            "s_waitcnt lgkmcnt(0)"
            : "=&v"(lo[0]), "=&v"(hi[0]), "=&v"(lo[1]), "=&v"(hi[1]), "=&v"(lo[2]), "=&v"(hi[2]), "=&v"(b0), "=&v"(b1)
            : "v"(tra[0][0]), "v"(tra[0][1]), "v"(tra[1][0]), "v"(tra[1][1]), "v"(tra[2][0]), "v"(tra[2][1]),
              "i"((c & 3) == 3 ? 0 : (c & 3) * TILEB), "v"(rdb[c & 3]), "i"((c >> 2) * 256),
              "i"((c >> 2) * 256 + 16 * ROWB)
            : "memory");
#pragma unroll
        for (int u = 0; u < 3; ++u) {
            u32x4 f = {lo[u][0], lo[u][1], hi[u][0], hi[u][1]};
            Wf[c][u] = __builtin_bit_cast(bf16x8, f);
        }
        if ((c & 3) == 2) {
#pragma unroll
            for (int u = 0; u < 3; ++u) { tra[u][0] += 3 * TILEB; tra[u][1] += 3 * TILEB; }
        } else if ((c & 3) == 3) {
#pragma unroll
            for (int u = 0; u < 3; ++u) { tra[u][0] -= 3 * TILEB; tra[u][1] -= 3 * TILEB; }
        }
#pragma unroll
        for (int u = 0; u < 3; ++u) {
            acc[0][u] = __builtin_amdgcn_mfma_f32_16x16x32_bf16(Wf[c][u], b0, c == 0 ? bvec[u] : acc[0][u], 0, 0, 0);
            acc[1][u] = __builtin_amdgcn_mfma_f32_16x16x32_bf16(Wf[c][u], b1, c == 0 ? bvec[u] : acc[1][u], 0, 0, 0);
        }
    }

    uint32_t wa[3];
#pragma unroll
    for (int u = 0; u < 3; ++u) {
        int b = 96 * wv + 24 * q + 8 * u;
        wa[u] = lds0 + r * ROWB + ((((b >> 4) ^ r) << 4) | (b & 15));
    }
    auto pack = [&](const f32x4 (&a)[2][3], u32x2 (&pk)[2][3]) {
#pragma unroll
        for (int mb = 0; mb < 2; ++mb)
#pragma unroll
            for (int u = 0; u < 3; ++u) {
                pk[mb][u][0] = cvt_pk_bf16(a[mb][u][0], a[mb][u][1]);
                pk[mb][u][1] = cvt_pk_bf16(a[mb][u][2], a[mb][u][3]);
            }
    };
    auto stores = [&](int t) {
        const char* sb = smem + (NXBUF + (t & 1)) * TILEB + wv * 3072 + lane * 16;
        char* dst = ow + (size_t)t * TILEB;
        u32x4 v[3];
#pragma unroll
        for (int m = 0; m < 3; ++m) v[m] = *(const u32x4*)(sb + m * 1024);
#pragma unroll
        for (int m = 0; m < 3; ++m) gstore16(dst, so[m], v[m]);
    };
#define STAGE_WRITE(PK, U, OO)                                                                    \
    do {                                                                                          \
        uint32_t a0_ = wa[U] + (OO);                                                              \
        asm volatile("ds_write_b64 %0, %1\n\tds_write_b64 %0, %2 offset:12288" ::"v"(a0_),        \
                     "v"(PK[0][U]), "v"(PK[1][U])                                                 \
                     : "memory");                                                                 \
    } while (0)

#pragma unroll 1
    for (int t = 1; t < NTILES; ++t) {
        u32x2 prev[2][3];
        pack(acc, prev);
        if (t >= 2 && t <= 5)
            asm volatile("s_waitcnt vmcnt(6) lgkmcnt(0)" ::: "memory");
        else if (t == 6)
            asm volatile("s_waitcnt vmcnt(3) lgkmcnt(0)" ::: "memory");
        else if (t == 7)
            asm volatile("s_waitcnt vmcnt(0) lgkmcnt(0)" ::: "memory");
        else
            asm volatile("s_waitcnt lgkmcnt(0)" ::: "memory");
        __builtin_amdgcn_s_barrier();
        const uint32_t oo = (NXBUF + ((t - 1) & 1)) * TILEB;
        bf16x8 bq[3][2];
        const uint32_t xo = (t & (NXBUF - 1)) * TILEB;
        uint32_t ra[4];
#pragma unroll
        for (int j = 0; j < 4; ++j) ra[j] = rdb[j] + xo;
#define RDFRAG(S)                                                                                           \
    asm volatile("ds_read_b128 %0, %2 offset:%3\n\tds_read_b128 %1, %2 offset:%4"                          \
                 : "=&v"(bq[(S) % 3][0]), "=&v"(bq[(S) % 3][1])                                               \
                 : "v"(ra[(S) & 3]), "i"(((S) >> 2) * 256), "i"(((S) >> 2) * 256 + 16 * ROWB)               \
                 : "memory")
        RDFRAG(0);
        RDFRAG(1);
#pragma unroll
        for (int s = 0; s < 12; ++s) {
            if (s + 2 < 12) RDFRAG(s + 2);
            if (s == 2 || s == 5)
                asm volatile("s_waitcnt lgkmcnt(6)" : "+v"(bq[s % 3][0]), "+v"(bq[s % 3][1])::"memory");
            else if (s == 3 || s == 4)
                asm volatile("s_waitcnt lgkmcnt(8)" : "+v"(bq[s % 3][0]), "+v"(bq[s % 3][1])::"memory");
            else if (s <= 9)
                asm volatile("s_waitcnt lgkmcnt(4)" : "+v"(bq[s % 3][0]), "+v"(bq[s % 3][1])::"memory");
            else if (s == 10)
                asm volatile("s_waitcnt lgkmcnt(2)" : "+v"(bq[s % 3][0]), "+v"(bq[s % 3][1])::"memory");
            else
                asm volatile("s_waitcnt lgkmcnt(0)" : "+v"(bq[s % 3][0]), "+v"(bq[s % 3][1])::"memory");
#pragma unroll
            for (int u = 0; u < 3; ++u) {
                acc[0][u] = __builtin_amdgcn_mfma_f32_16x16x32_bf16(Wf[s][u], bq[s % 3][0], s == 0 ? bvec[u] : acc[0][u], 0, 0, 0);
                acc[1][u] = __builtin_amdgcn_mfma_f32_16x16x32_bf16(Wf[s][u], bq[s % 3][1], s == 0 ? bvec[u] : acc[1][u], 0, 0, 0);
            }
            if (s >= 1 && s <= 3) STAGE_WRITE(prev, s - 1, oo);
            if (s == 5 && t + 3 < NTILES) issue_dma(t + 3);
        }
        if (t >= 2) stores(t - 2);
    }
    asm volatile("s_waitcnt lgkmcnt(0)" ::: "memory");
    __builtin_amdgcn_s_barrier();
    stores(NTILES - 2);
    {
        u32x2 pk[2][3];
        pack(acc, pk);
        const uint32_t oo = (NXBUF + ((NTILES - 1) & 1)) * TILEB;
#pragma unroll
        for (int u = 0; u < 3; ++u) STAGE_WRITE(pk, u, oo);
    }
    asm volatile("s_waitcnt lgkmcnt(0)" ::: "memory");
    __builtin_amdgcn_s_barrier();
    stores(NTILES - 1);
}

__global__ void conv1x1_fallback(const __hip_bfloat16* __restrict__ x, const __hip_bfloat16* __restrict__ w,
                                 const __hip_bfloat16* __restrict__ b, __hip_bfloat16* __restrict__ out, int M) {
    int n = threadIdx.x;
    for (int row = blockIdx.x; row < M; row += gridDim.x) {
        float acc = 0.f;
        for (int k = 0; k < CC; ++k) acc += __bfloat162float(x[(size_t)row * CC + k]) * __bfloat162float(w[k * CC + n]);
        out[(size_t)row * CC + n] = __float2bfloat16(acc + __bfloat162float(b[n]));
    }
}

extern "C" void kernel_launch(void* const* d_in, const int* in_sizes, int n_in, void* d_out, int out_size, void* d_ws,
                              size_t ws_size, hipStream_t stream) {
    const int M = in_sizes[0] / CC;
    if (M % WG_ROWS == 0) {
        conv1x1_kernel<<<M / WG_ROWS, 512, 0, stream>>>((const char*)d_in[0], (const char*)d_in[1],
                                                        (const char*)d_in[2], (char*)d_out);
    } else {
        conv1x1_fallback<<<2048, CC, 0, stream>>>((const __hip_bfloat16*)d_in[0], (const __hip_bfloat16*)d_in[1],
                                                  (const __hip_bfloat16*)d_in[2], (__hip_bfloat16*)d_out, M);
    }
}
```

```cpp
#include <hip/hip_runtime.h>
#include <hip/hip_bf16.h>
#include <stdint.h>

typedef __attribute__((ext_vector_type(8))) short bf16x8;
typedef __attribute__((ext_vector_type(4))) float f32x4;
typedef __attribute__((ext_vector_type(2))) float f32x2;
typedef __attribute__((ext_vector_type(2))) __bf16 bf16x2;
typedef __attribute__((ext_vector_type(4))) uint32_t u32x4;
typedef __attribute__((ext_vector_type(2))) uint32_t u32x2;

#define CC 384
#define ROWB 768
#define TROWS 32
#define TILEB (TROWS * ROWB)
#define NXBUF 4
#define WG_ROWS 256
#define NTILES (WG_ROWS / TROWS)
#define LDS_BYTES ((NXBUF + 2) * TILEB)

__device__ __forceinline__ uint32_t cvt_pk_bf16(float a, float b) {
    f32x2 v = {a, b};
    bf16x2 h = __builtin_convertvector(v, bf16x2);
    return __builtin_bit_cast(uint32_t, h);
}

__device__ __forceinline__ void glds16_nt(const void* sbase, uint32_t voff, uint32_t lds_dst) {
    uint32_t keep;
    asm volatile(
        "s_mov_b32 %0, m0\n\t"
        "s_mov_b32 m0, %3\n\t"
        "s_nop 0\n\t"
        "global_load_lds_dwordx4 %1, %2 nt\n\t"
        "s_mov_b32 m0, %0"
        : "=&s"(keep)
        : "v"(voff), "s"(sbase), "s"(lds_dst)
        : "memory");
}
__device__ __forceinline__ void glds16(const void* sbase, uint32_t voff, uint32_t lds_dst) {
    uint32_t keep;
    asm volatile(
        "s_mov_b32 %0, m0\n\t"
        "s_mov_b32 m0, %3\n\t"
        "s_nop 0\n\t"
        "global_load_lds_dwordx4 %1, %2\n\t"
        "s_mov_b32 m0, %0"
        : "=&s"(keep)
        : "v"(voff), "s"(sbase), "s"(lds_dst)
        : "memory");
}

__device__ __forceinline__ void gstore16(void* sbase, uint32_t voff, u32x4 v) {
    asm volatile("global_store_dwordx4 %0, %1, %2 nt\n\ts_nop 1" ::"v"(voff), "v"(v), "s"(sbase) : "memory");
}

__global__ __launch_bounds__(512, 2) void conv1x1_kernel(const char* __restrict__ x, const char* __restrict__ w,
                                                         const char* __restrict__ bias, char* __restrict__ out) {
    __shared__ __attribute__((aligned(1024))) char smem[LDS_BYTES];
    const int tid = threadIdx.x;
    const int lane = tid & 63;
    const int wv = __builtin_amdgcn_readfirstlane(tid >> 6);
    const int q = lane >> 4, r = lane & 15;
    const uint32_t lds0 = (uint32_t)(size_t)(__attribute__((address_space(3))) char*)smem;

    uint32_t pref = 0;
    {
        int line = (wv & 1) * 64 + lane;
        line = line < 72 ? line : 71;
        const char* pa = w + ((blockIdx.x >> 3) & 31) * 9216 + line * 128;
        if (wv < 2) asm volatile("global_load_dword %0, %1, off" : "=v"(pref) : "v"(pa) : "memory");
    }
    u32x2 braw[3];
    {
        const char* bp = bias + 96 * wv + 24 * q;
        asm volatile(
            "global_load_dwordx2 %0, %3, off\n\tglobal_load_dwordx2 %1, %3, off offset:8\n\t"
            "global_load_dwordx2 %2, %3, off offset:16"
            : "=&v"(braw[0]), "=&v"(braw[1]), "=&v"(braw[2])
            : "v"(bp)
            : "memory");
    }
    f32x4 bvec[3];

    uint32_t so[3];
#pragma unroll
    for (int m = 0; m < 3; ++m) {
        int p = 64 * m + lane;
        int rowl = p / 48, c = p % 48;
        int rr = 4 * (wv & 3) + rowl;
        so[m] = rowl * ROWB + ((c ^ rr) * 16);
    }
    const size_t wg_off = (size_t)blockIdx.x * WG_ROWS * ROWB + (size_t)wv * 4 * ROWB;
    const char* xw = x + wg_off;
    char* ow = out + wg_off;
    const char* ww = w + wv * 4 * ROWB;

    auto issue_dma = [&](int t) {
        const char* src = xw + (size_t)t * TILEB;
        uint32_t dst = lds0 + (t & (NXBUF - 1)) * TILEB + wv * 3072;
#pragma unroll
        for (int m = 0; m < 3; ++m) glds16_nt(src, so[m], dst + m * 1024);
    };
    auto issue_w = [&](int c) {
        const char* src = ww + (size_t)c * TILEB;
        uint32_t dst = lds0 + (2 + (c & 3)) * TILEB + wv * 3072;
#pragma unroll
        for (int m = 0; m < 3; ++m) glds16(src, so[m], dst + m * 1024);
    };

    issue_dma(0);
    issue_dma(1);
    issue_w(0);
    issue_w(1);
    issue_w(2);

    uint32_t rdb[4];
#pragma unroll
    for (int j = 0; j < 4; ++j) rdb[j] = lds0 + r * ROWB + (((4 * j + q) ^ r) * 16);
    uint32_t tra[3][2];
    {
        const int a = r >> 2, p = r & 3;
#pragma unroll
        for (int u = 0; u < 3; ++u)
#pragma unroll
            for (int hf = 0; hf < 2; ++hf) {
                int k = 8 * q + 4 * hf + a;
                int b = 96 * wv + 24 * p + 8 * u;
                tra[u][hf] = lds0 + 2 * TILEB + k * ROWB + ((((b >> 4) ^ (k & 15)) << 4) | (b & 15));
            }
    }

    bf16x8 Wf[12][3];
    f32x4 acc[2][3];
#pragma unroll
    for (int c = 0; c < 12; ++c) {
        asm volatile("s_waitcnt vmcnt(6) lgkmcnt(0)" ::: "memory");
        __builtin_amdgcn_s_barrier();
        if (c == 0) {
            asm volatile("" : "+v"(braw[0]), "+v"(braw[1]), "+v"(braw[2]), "+v"(pref));
#pragma unroll
            for (int u = 0; u < 3; ++u) {
                bvec[u][0] = __uint_as_float(braw[u][0] << 16);
                bvec[u][1] = __uint_as_float(braw[u][0] & 0xffff0000u);
                bvec[u][2] = __uint_as_float(braw[u][1] << 16);
                bvec[u][3] = __uint_as_float(braw[u][1] & 0xffff0000u);
            }
        }
        u32x2 lo[3], hi[3];
        bf16x8 b0, b1;
        asm volatile(
            "ds_read_b64_tr_b16 %0, %8 offset:%14\n\t"
            "ds_read_b64_tr_b16 %1, %9 offset:%14\n\t"
            "ds_read_b64_tr_b16 %2, %10 offset:%14\n\t"
            "ds_read_b64_tr_b16 %3, %11 offset:%14\n\t"
            "ds_read_b64_tr_b16 %4, %12 offset:%14\n\t"
            "ds_read_b64_tr_b16 %5, %13 offset:%14\n\t"
            "ds_read_b128 %6, %15 offset:%16\n\t"
            "ds_read_b128 %7, %15 offset:%17"
            : "=&v"(lo[0]), "=&v"(hi[0]), "=&v"(lo[1]), "=&v"(hi[1]), "=&v"(lo[2]), "=&v"(hi[2]), "=&v"(b0), "=&v"(b1)
            : "v"(tra[0][0]), "v"(tra[0][1]), "v"(tra[1][0]), "v"(tra[1][1]), "v"(tra[2][0]), "v"(tra[2][1]),
              "i"((c & 3) == 3 ? 0 : (c & 3) * TILEB), "v"(rdb[c & 3]), "i"((c >> 2) * 256),
              "i"((c >> 2) * 256 + 16 * ROWB)
            : "memory");
        if (c + 3 < 12) issue_w(c + 3);
        else if (c == 9 || c == 10) issue_dma(c - 7);
        asm volatile("s_waitcnt lgkmcnt(0)"
                     : "+v"(lo[0]), "+v"(hi[0]), "+v"(lo[1]), "+v"(hi[1]), "+v"(lo[2]), "+v"(hi[2]), "+v"(b0), "+v"(b1)::"memory");
#pragma unroll
        for (int u = 0; u < 3; ++u) {
            u32x4 f = {lo[u][0], lo[u][1], hi[u][0], hi[u][1]};
            Wf[c][u] = __builtin_bit_cast(bf16x8, f);
        }
        if ((c & 3) == 2) {
#pragma unroll
            for (int u = 0; u < 3; ++u) { tra[u][0] += 3 * TILEB; tra[u][1] += 3 * TILEB; }
        } else if ((c & 3) == 3) {
#pragma unroll
            for (int u = 0; u < 3; ++u) { tra[u][0] -= 3 * TILEB; tra[u][1] -= 3 * TILEB; }
        }
#pragma unroll
        for (int u = 0; u < 3; ++u) {
            acc[0][u] = __builtin_amdgcn_mfma_f32_16x16x32_bf16(Wf[c][u], b0, c == 0 ? bvec[u] : acc[0][u], 0, 0, 0);
            acc[1][u] = __builtin_amdgcn_mfma_f32_16x16x32_bf16(Wf[c][u], b1, c == 0 ? bvec[u] : acc[1][u], 0, 0, 0);
        }
    }

    uint32_t wa[3];
#pragma unroll
    for (int u = 0; u < 3; ++u) {
        int b = 96 * wv + 24 * q + 8 * u;
        wa[u] = lds0 + r * ROWB + ((((b >> 4) ^ r) << 4) | (b & 15));
    }
    auto pack = [&](const f32x4 (&a)[2][3], u32x2 (&pk)[2][3]) {
#pragma unroll
        for (int mb = 0; mb < 2; ++mb)
#pragma unroll
            for (int u = 0; u < 3; ++u) {
                pk[mb][u][0] = cvt_pk_bf16(a[mb][u][0], a[mb][u][1]);
                pk[mb][u][1] = cvt_pk_bf16(a[mb][u][2], a[mb][u][3]);
            }
    };
    auto stores = [&](int t) {
        const char* sb = smem + (NXBUF + (t & 1)) * TILEB + wv * 3072 + lane * 16;
        char* dst = ow + (size_t)t * TILEB;
        u32x4 v[3];
#pragma unroll
        for (int m = 0; m < 3; ++m) v[m] = *(const u32x4*)(sb + m * 1024);
#pragma unroll
        for (int m = 0; m < 3; ++m) gstore16(dst, so[m], v[m]);
    };
#define STAGE_WRITE(PK, U, OO)                                                                    \
    do {                                                                                          \
        uint32_t a0_ = wa[U] + (OO);                                                              \
        asm volatile("ds_write_b64 %0, %1\n\tds_write_b64 %0, %2 offset:12288" ::"v"(a0_),        \
                     "v"(PK[0][U]), "v"(PK[1][U])                                                 \
                     : "memory");                                                                 \
    } while (0)

#pragma unroll 1
    for (int t = 1; t < NTILES; ++t) {
        u32x2 prev[2][3];
        pack(acc, prev);
        if (t >= 2 && t <= 5)
            asm volatile("s_waitcnt vmcnt(6) lgkmcnt(0)" ::: "memory");
        else if (t == 6)
            asm volatile("s_waitcnt vmcnt(3) lgkmcnt(0)" ::: "memory");
        else if (t == 7)
            asm volatile("s_waitcnt vmcnt(0) lgkmcnt(0)" ::: "memory");
        else
            asm volatile("s_waitcnt lgkmcnt(0)" ::: "memory");
        __builtin_amdgcn_s_barrier();
        const uint32_t oo = (NXBUF + ((t - 1) & 1)) * TILEB;
        bf16x8 bq[3][2];
        const uint32_t xo = (t & (NXBUF - 1)) * TILEB;
        uint32_t ra[4];
#pragma unroll
        for (int j = 0; j < 4; ++j) ra[j] = rdb[j] + xo;
#define RDFRAG(S)                                                                                           \
    asm volatile("ds_read_b128 %0, %2 offset:%3\n\tds_read_b128 %1, %2 offset:%4"                          \
                 : "=&v"(bq[(S) % 3][0]), "=&v"(bq[(S) % 3][1])                                               \
                 : "v"(ra[(S) & 3]), "i"(((S) >> 2) * 256), "i"(((S) >> 2) * 256 + 16 * ROWB)               \
                 : "memory")
        auto vmem_work = [&]() {
            if (t >= 2) stores(t - 2);
            if (t + 3 < NTILES) issue_dma(t + 3);
        };
        if (wv >= 4) vmem_work();
        RDFRAG(0);
        RDFRAG(1);
#pragma unroll
        for (int s = 0; s < 12; ++s) {
            if (s + 2 < 12) RDFRAG(s + 2);
            if (s == 2 || s == 5)
                asm volatile("s_waitcnt lgkmcnt(6)" : "+v"(bq[s % 3][0]), "+v"(bq[s % 3][1])::"memory");
            else if (s == 3 || s == 4)
                asm volatile("s_waitcnt lgkmcnt(8)" : "+v"(bq[s % 3][0]), "+v"(bq[s % 3][1])::"memory");
            else if (s <= 9)
                asm volatile("s_waitcnt lgkmcnt(4)" : "+v"(bq[s % 3][0]), "+v"(bq[s % 3][1])::"memory");
            else if (s == 10)
                asm volatile("s_waitcnt lgkmcnt(2)" : "+v"(bq[s % 3][0]), "+v"(bq[s % 3][1])::"memory");
            else
                asm volatile("s_waitcnt lgkmcnt(0)" : "+v"(bq[s % 3][0]), "+v"(bq[s % 3][1])::"memory");
#pragma unroll
            for (int u = 0; u < 3; ++u) {
                acc[0][u] = __builtin_amdgcn_mfma_f32_16x16x32_bf16(Wf[s][u], bq[s % 3][0], s == 0 ? bvec[u] : acc[0][u], 0, 0, 0);
                acc[1][u] = __builtin_amdgcn_mfma_f32_16x16x32_bf16(Wf[s][u], bq[s % 3][1], s == 0 ? bvec[u] : acc[1][u], 0, 0, 0);
            }
            if (s >= 1 && s <= 3) STAGE_WRITE(prev, s - 1, oo);
        }
        if (wv < 4) vmem_work();
    }
    asm volatile("s_waitcnt lgkmcnt(0)" ::: "memory");
    __builtin_amdgcn_s_barrier();
    stores(NTILES - 2);
    {
        u32x2 pk[2][3];
        pack(acc, pk);
        const uint32_t oo = (NXBUF + ((NTILES - 1) & 1)) * TILEB;
#pragma unroll
        for (int u = 0; u < 3; ++u) STAGE_WRITE(pk, u, oo);
    }
    asm volatile("s_waitcnt lgkmcnt(0)" ::: "memory");
    __builtin_amdgcn_s_barrier();
    stores(NTILES - 1);
}

__global__ void conv1x1_fallback(const __hip_bfloat16* __restrict__ x, const __hip_bfloat16* __restrict__ w,
                                 const __hip_bfloat16* __restrict__ b, __hip_bfloat16* __restrict__ out, int M) {
    int n = threadIdx.x;
    for (int row = blockIdx.x; row < M; row += gridDim.x) {
        float acc = 0.f;
        for (int k = 0; k < CC; ++k) acc += __bfloat162float(x[(size_t)row * CC + k]) * __bfloat162float(w[k * CC + n]);
        out[(size_t)row * CC + n] = __float2bfloat16(acc + __bfloat162float(b[n]));
    }
}

extern "C" void kernel_launch(void* const* d_in, const int* in_sizes, int n_in, void* d_out, int out_size, void* d_ws,
                              size_t ws_size, hipStream_t stream) {
    const int M = in_sizes[0] / CC;
    if (M % WG_ROWS == 0) {
        conv1x1_kernel<<<M / WG_ROWS, 512, 0, stream>>>((const char*)d_in[0], (const char*)d_in[1],
                                                        (const char*)d_in[2], (char*)d_out);
    } else {
        conv1x1_fallback<<<2048, CC, 0, stream>>>((const __hip_bfloat16*)d_in[0], (const __hip_bfloat16*)d_in[1],
                                                  (const __hip_bfloat16*)d_in[2], (__hip_bfloat16*)d_out, M);
    }
}
```

```cpp
#include <hip/hip_runtime.h>
#include <hip/hip_bf16.h>
#include <stdint.h>

typedef __attribute__((ext_vector_type(8))) short bf16x8;
typedef __attribute__((ext_vector_type(4))) float f32x4;
typedef __attribute__((ext_vector_type(2))) float f32x2;
typedef __attribute__((ext_vector_type(2))) __bf16 bf16x2;
typedef __attribute__((ext_vector_type(4))) uint32_t u32x4;
typedef __attribute__((ext_vector_type(2))) uint32_t u32x2;

#define CC 384
#define ROWB 768
#define TROWS 32
#define TILEB (TROWS * ROWB)
#define NXBUF 4
#define WG_ROWS 256
#define NTILES (WG_ROWS / TROWS)
#define LDS_BYTES ((NXBUF + 2) * TILEB)

__device__ __forceinline__ uint32_t cvt_pk_bf16(float a, float b) {
    f32x2 v = {a, b};
    bf16x2 h = __builtin_convertvector(v, bf16x2);
    return __builtin_bit_cast(uint32_t, h);
}

__device__ __forceinline__ void glds16_nt(const void* sbase, uint32_t voff, uint32_t lds_dst) {
    uint32_t keep;
    asm volatile(
        "s_mov_b32 %0, m0\n\t"
        "s_mov_b32 m0, %3\n\t"
        "s_nop 0\n\t"
        "global_load_lds_dwordx4 %1, %2 nt\n\t"
        "s_mov_b32 m0, %0"
        : "=&s"(keep)
        : "v"(voff), "s"(sbase), "s"(lds_dst)
        : "memory");
}
__device__ __forceinline__ void glds16(const void* sbase, uint32_t voff, uint32_t lds_dst) {
    uint32_t keep;
    asm volatile(
        "s_mov_b32 %0, m0\n\t"
        "s_mov_b32 m0, %3\n\t"
        "s_nop 0\n\t"
        "global_load_lds_dwordx4 %1, %2\n\t"
        "s_mov_b32 m0, %0"
        : "=&s"(keep)
        : "v"(voff), "s"(sbase), "s"(lds_dst)
        : "memory");
}

__device__ __forceinline__ void gstore16(void* sbase, uint32_t voff, u32x4 v) {
    asm volatile("global_store_dwordx4 %0, %1, %2 nt\n\ts_nop 1" ::"v"(voff), "v"(v), "s"(sbase) : "memory");
}

__global__ __launch_bounds__(512, 2) void conv1x1_kernel(const char* __restrict__ x, const char* __restrict__ w,
                                                         const char* __restrict__ bias, char* __restrict__ out) {
    __shared__ __attribute__((aligned(1024))) char smem[LDS_BYTES];
    const int tid = threadIdx.x;
    const int lane = tid & 63;
    const int wv = __builtin_amdgcn_readfirstlane(tid >> 6);
    const int q = lane >> 4, r = lane & 15;
    const uint32_t lds0 = (uint32_t)(size_t)(__attribute__((address_space(3))) char*)smem;

    uint32_t pref = 0;
    {
        int line = (wv & 1) * 64 + lane;
        line = line < 72 ? line : 71;
        const char* pa = w + ((blockIdx.x >> 3) & 31) * 9216 + line * 128;
        if (wv < 2) asm volatile("global_load_dword %0, %1, off" : "=v"(pref) : "v"(pa) : "memory");
    }
    u32x2 braw[3];
    {
        const char* bp = bias + 96 * wv + 24 * q;
        asm volatile(
            "global_load_dwordx2 %0, %3, off\n\tglobal_load_dwordx2 %1, %3, off offset:8\n\t"
            "global_load_dwordx2 %2, %3, off offset:16"
            : "=&v"(braw[0]), "=&v"(braw[1]), "=&v"(braw[2])
            : "v"(bp)
            : "memory");
    }
    f32x4 bvec[3];

    uint32_t so[3];
#pragma unroll
    for (int m = 0; m < 3; ++m) {
        int p = 64 * m + lane;
        int rowl = p / 48, c = p % 48;
        int rr = 4 * (wv & 3) + rowl;
        so[m] = rowl * ROWB + ((c ^ rr) * 16);
    }
    const size_t wg_off = (size_t)blockIdx.x * WG_ROWS * ROWB + (size_t)wv * 4 * ROWB;
    const char* xw = x + wg_off;
    char* ow = out + wg_off;
    const char* ww = w + wv * 4 * ROWB;

    auto issue_dma = [&](int t) {
        const char* src = xw + (size_t)t * TILEB;
        uint32_t dst = lds0 + (t & (NXBUF - 1)) * TILEB + wv * 3072;
#pragma unroll
        for (int m = 0; m < 3; ++m) glds16_nt(src, so[m], dst + m * 1024);
    };
    auto issue_w = [&](int c) {
        const char* src = ww + (size_t)c * TILEB;
        uint32_t dst = lds0 + (2 + (c & 3)) * TILEB + wv * 3072;
#pragma unroll
        for (int m = 0; m < 3; ++m) glds16(src, so[m], dst + m * 1024);
    };

    issue_dma(0);
    issue_w(0);
    issue_w(1);
    issue_w(2);

    uint32_t rdb[4];
#pragma unroll
    for (int j = 0; j < 4; ++j) rdb[j] = lds0 + r * ROWB + (((4 * j + q) ^ r) * 16);
    uint32_t tra[3][2];
    {
        const int a = r >> 2, p = r & 3;
#pragma unroll
        for (int u = 0; u < 3; ++u)
#pragma unroll
            for (int hf = 0; hf < 2; ++hf) {
                int k = 8 * q + 4 * hf + a;
                int b = 96 * wv + 24 * p + 8 * u;
                tra[u][hf] = lds0 + 2 * TILEB + k * ROWB + ((((b >> 4) ^ (k & 15)) << 4) | (b & 15));
            }
    }

    bf16x8 Wf[12][3];
    f32x4 acc[2][3];
    bf16x8 xb[2][2];
#pragma unroll
    for (int c = 0; c <= 12; ++c) {
        u32x2 lo[3], hi[3];
        if (c < 12) {
            asm volatile("s_waitcnt vmcnt(6) lgkmcnt(0)" ::: "memory");
            __builtin_amdgcn_s_barrier();
            if (c == 0) {
                asm volatile("" : "+v"(braw[0]), "+v"(braw[1]), "+v"(braw[2]), "+v"(pref));
#pragma unroll
                for (int u = 0; u < 3; ++u) {
                    bvec[u][0] = __uint_as_float(braw[u][0] << 16);
                    bvec[u][1] = __uint_as_float(braw[u][0] & 0xffff0000u);
                    bvec[u][2] = __uint_as_float(braw[u][1] << 16);
                    bvec[u][3] = __uint_as_float(braw[u][1] & 0xffff0000u);
                }
            }
            asm volatile(
                "ds_read_b64_tr_b16 %0, %8 offset:%14\n\t"
                "ds_read_b64_tr_b16 %1, %9 offset:%14\n\t"
                "ds_read_b64_tr_b16 %2, %10 offset:%14\n\t"
                "ds_read_b64_tr_b16 %3, %11 offset:%14\n\t"
                "ds_read_b64_tr_b16 %4, %12 offset:%14\n\t"
                "ds_read_b64_tr_b16 %5, %13 offset:%14\n\t"
                "ds_read_b128 %6, %15 offset:%16\n\t"
                "ds_read_b128 %7, %15 offset:%17"
                : "=&v"(lo[0]), "=&v"(hi[0]), "=&v"(lo[1]), "=&v"(hi[1]), "=&v"(lo[2]), "=&v"(hi[2]), "=&v"(xb[c & 1][0]),
                  "=&v"(xb[c & 1][1])
                : "v"(tra[0][0]), "v"(tra[0][1]), "v"(tra[1][0]), "v"(tra[1][1]), "v"(tra[2][0]), "v"(tra[2][1]),
                  "i"((c & 3) == 3 ? 0 : (c & 3) * TILEB), "v"(rdb[c & 3]), "i"((c >> 2) * 256),
                  "i"((c >> 2) * 256 + 16 * ROWB)
                : "memory");
            if (c + 3 < 12) issue_w(c + 3);
            else if (c == 9) issue_dma(1);
            else if (c == 10) issue_dma(2);
            else if (c == 11) issue_dma(3);
        }
        if (c >= 1) {
#pragma unroll
            for (int u = 0; u < 3; ++u) {
                acc[0][u] = __builtin_amdgcn_mfma_f32_16x16x32_bf16(Wf[c - 1][u], xb[(c - 1) & 1][0], c == 1 ? bvec[u] : acc[0][u], 0, 0, 0);
                acc[1][u] = __builtin_amdgcn_mfma_f32_16x16x32_bf16(Wf[c - 1][u], xb[(c - 1) & 1][1], c == 1 ? bvec[u] : acc[1][u], 0, 0, 0);
            }
        }
        if (c < 12) {
            asm volatile("s_waitcnt lgkmcnt(0)"
                         : "+v"(lo[0]), "+v"(hi[0]), "+v"(lo[1]), "+v"(hi[1]), "+v"(lo[2]), "+v"(hi[2]), "+v"(xb[c & 1][0]),
                           "+v"(xb[c & 1][1])::"memory");
#pragma unroll
            for (int u = 0; u < 3; ++u) {
                u32x4 f = {lo[u][0], lo[u][1], hi[u][0], hi[u][1]};
                Wf[c][u] = __builtin_bit_cast(bf16x8, f);
            }
            if ((c & 3) == 2) {
#pragma unroll
                for (int u = 0; u < 3; ++u) { tra[u][0] += 3 * TILEB; tra[u][1] += 3 * TILEB; }
            } else if ((c & 3) == 3) {
#pragma unroll
                for (int u = 0; u < 3; ++u) { tra[u][0] -= 3 * TILEB; tra[u][1] -= 3 * TILEB; }
            }
        }
    }

    uint32_t wa[3];
#pragma unroll
    for (int u = 0; u < 3; ++u) {
        int b = 96 * wv + 24 * q + 8 * u;
        wa[u] = lds0 + r * ROWB + ((((b >> 4) ^ r) << 4) | (b & 15));
    }
    auto pack = [&](const f32x4 (&a)[2][3], u32x2 (&pk)[2][3]) {
#pragma unroll
        for (int mb = 0; mb < 2; ++mb)
#pragma unroll
            for (int u = 0; u < 3; ++u) {
                pk[mb][u][0] = cvt_pk_bf16(a[mb][u][0], a[mb][u][1]);
                pk[mb][u][1] = cvt_pk_bf16(a[mb][u][2], a[mb][u][3]);
            }
    };
    auto stores = [&](int t) {
        const char* sb = smem + (NXBUF + (t & 1)) * TILEB + wv * 3072 + lane * 16;
        char* dst = ow + (size_t)t * TILEB;
        u32x4 v[3];
#pragma unroll
        for (int m = 0; m < 3; ++m) v[m] = *(const u32x4*)(sb + m * 1024);
#pragma unroll
        for (int m = 0; m < 3; ++m) gstore16(dst, so[m], v[m]);
    };
#define STAGE_WRITE(PK, U, OO)                                                                    \
    do {                                                                                          \
        uint32_t a0_ = wa[U] + (OO);                                                              \
        asm volatile("ds_write_b64 %0, %1\n\tds_write_b64 %0, %2 offset:12288" ::"v"(a0_),        \
                     "v"(PK[0][U]), "v"(PK[1][U])                                                 \
                     : "memory");                                                                 \
    } while (0)

#pragma unroll 1
    for (int t = 1; t < NTILES; ++t) {
        u32x2 prev[2][3];
        pack(acc, prev);
        if (t <= 5)
            asm volatile("s_waitcnt vmcnt(6) lgkmcnt(0)" ::: "memory");
        else if (t == 6)
            asm volatile("s_waitcnt vmcnt(3) lgkmcnt(0)" ::: "memory");
        else if (t == 7)
            asm volatile("s_waitcnt vmcnt(0) lgkmcnt(0)" ::: "memory");
        else
            asm volatile("s_waitcnt lgkmcnt(0)" ::: "memory");
        __builtin_amdgcn_s_barrier();
        const uint32_t oo = (NXBUF + ((t - 1) & 1)) * TILEB;
        bf16x8 bq[3][2];
        const uint32_t xo = (t & (NXBUF - 1)) * TILEB;
        uint32_t ra[4];
#pragma unroll
        for (int j = 0; j < 4; ++j) ra[j] = rdb[j] + xo;
#define RDFRAG(S)                                                                                           \
    asm volatile("ds_read_b128 %0, %2 offset:%3\n\tds_read_b128 %1, %2 offset:%4"                          \
                 : "=&v"(bq[(S) % 3][0]), "=&v"(bq[(S) % 3][1])                                               \
                 : "v"(ra[(S) & 3]), "i"(((S) >> 2) * 256), "i"(((S) >> 2) * 256 + 16 * ROWB)               \
                 : "memory")
        auto vmem_work = [&]() {
            if (t >= 2) stores(t - 2);
            if (t + 3 < NTILES) issue_dma(t + 3);
        };
        if (wv >= 4) vmem_work();
        RDFRAG(0);
        RDFRAG(1);
#pragma unroll
        for (int s = 0; s < 12; ++s) {
            if (s + 2 < 12) RDFRAG(s + 2);
            if (s == 2 || s == 5)
                asm volatile("s_waitcnt lgkmcnt(6)" : "+v"(bq[s % 3][0]), "+v"(bq[s % 3][1])::"memory");
            else if (s == 3 || s == 4)
                asm volatile("s_waitcnt lgkmcnt(8)" : "+v"(bq[s % 3][0]), "+v"(bq[s % 3][1])::"memory");
            else if (s <= 9)
                asm volatile("s_waitcnt lgkmcnt(4)" : "+v"(bq[s % 3][0]), "+v"(bq[s % 3][1])::"memory");
            else if (s == 10)
                asm volatile("s_waitcnt lgkmcnt(2)" : "+v"(bq[s % 3][0]), "+v"(bq[s % 3][1])::"memory");
            else
                asm volatile("s_waitcnt lgkmcnt(0)" : "+v"(bq[s % 3][0]), "+v"(bq[s % 3][1])::"memory");
#pragma unroll
            for (int u = 0; u < 3; ++u) {
                acc[0][u] = __builtin_amdgcn_mfma_f32_16x16x32_bf16(Wf[s][u], bq[s % 3][0], s == 0 ? bvec[u] : acc[0][u], 0, 0, 0);
                acc[1][u] = __builtin_amdgcn_mfma_f32_16x16x32_bf16(Wf[s][u], bq[s % 3][1], s == 0 ? bvec[u] : acc[1][u], 0, 0, 0);
            }
            if (s >= 1 && s <= 3) STAGE_WRITE(prev, s - 1, oo);
        }
        if (wv < 4) vmem_work();
    }
    asm volatile("s_waitcnt lgkmcnt(0)" ::: "memory");
    __builtin_amdgcn_s_barrier();
    stores(NTILES - 2);
    {
        u32x2 pk[2][3];
        pack(acc, pk);
        const uint32_t oo = (NXBUF + ((NTILES - 1) & 1)) * TILEB;
#pragma unroll
        for (int u = 0; u < 3; ++u) STAGE_WRITE(pk, u, oo);
    }
    asm volatile("s_waitcnt lgkmcnt(0)" ::: "memory");
    __builtin_amdgcn_s_barrier();
    stores(NTILES - 1);
}

__global__ void conv1x1_fallback(const __hip_bfloat16* __restrict__ x, const __hip_bfloat16* __restrict__ w,
                                 const __hip_bfloat16* __restrict__ b, __hip_bfloat16* __restrict__ out, int M) {
    int n = threadIdx.x;
    for (int row = blockIdx.x; row < M; row += gridDim.x) {
        float acc = 0.f;
        for (int k = 0; k < CC; ++k) acc += __bfloat162float(x[(size_t)row * CC + k]) * __bfloat162float(w[k * CC + n]);
        out[(size_t)row * CC + n] = __float2bfloat16(acc + __bfloat162float(b[n]));
    }
}

extern "C" void kernel_launch(void* const* d_in, const int* in_sizes, int n_in, void* d_out, int out_size, void* d_ws,
                              size_t ws_size, hipStream_t stream) {
    const int M = in_sizes[0] / CC;
    if (M % WG_ROWS == 0) {
        conv1x1_kernel<<<M / WG_ROWS, 512, 0, stream>>>((const char*)d_in[0], (const char*)d_in[1],
                                                        (const char*)d_in[2], (char*)d_out);
    } else {
        conv1x1_fallback<<<2048, CC, 0, stream>>>((const __hip_bfloat16*)d_in[0], (const __hip_bfloat16*)d_in[1],
                                                  (const __hip_bfloat16*)d_in[2], (__hip_bfloat16*)d_out, M);
    }
}
```
